```python
import torch

def _softmax(x, axis=-1):
    return torch.softmax(x, dim=axis)
def _gelu(x):
    return torch.nn.functional.gelu(x, approximate="tanh")
def _silu(x):
    return torch.nn.functional.silu(x)
def _sigmoid(x):
    return torch.sigmoid(x)
def _layernorm(x, axis=-1, eps=1e-5):
    m = x.mean(dim=axis, keepdim=True)
    v = x.var(dim=axis, keepdim=True, unbiased=False)
    return (x - m) / torch.sqrt(v + eps)
def _scatter_add(tgt, idx, src, axis=0):
    return tgt.clone().index_add_(axis, idx, src)
def _segment_sum(src, seg, n):
    out = torch.zeros((n,) + src.shape[1:], dtype=src.dtype)
    return out.index_add_(0, seg, src)
def _pad(x, pw):
    flat = tuple(v for pair in reversed(pw) for v in pair)
    return torch.nn.functional.pad(x, flat)

kernel_name = 'interleave_layers__mha_block_cached__dense_activation_gelu__decode'

def setup_inputs():

    _w = (lambda a: (lambda t: t.to(torch.bfloat16) if t.is_floating_point() else t)(torch.as_tensor(a)))
    def _A_setup():
        B, Sq, D = 1, 1, 512
        H, Sk, Dh = 32, 4096, 128
        return {
            "x": _w((lambda *s: torch.randn(*s, dtype=torch.bfloat16) * 0.05)(B, Sq, D)),
            "k_cache": _w((lambda *s: torch.randn(*s, dtype=torch.bfloat16) * 0.8)(B, H, Sk, Dh)),
            "v_cache": _w((lambda *s: torch.randn(*s, dtype=torch.bfloat16) * 1.6)(B, H, Sk, Dh)),
            "wq": _w((lambda *s: torch.randn(*s, dtype=torch.bfloat16) * 0.8)(D, H * Dh) * (D ** -0.5)),
            "wo": _w((lambda *s: torch.randn(*s, dtype=torch.bfloat16) * 1.6)(H * Dh, D) * ((H * Dh) ** -0.5)),
        }
    _A_in = _A_setup()

    def _B_setup():
        B, S, D, F = 1, 1, 512, 1376
        return {
            "x": _w((lambda *s: torch.randn(*s, dtype=torch.bfloat16) * 0.05)(B, S, D)),
            "w1": _w((lambda *s: torch.randn(*s, dtype=torch.bfloat16) * 0.4)(D, F) * (D ** -0.5)),
            "b1": _w((lambda *s: torch.randn(*s, dtype=torch.bfloat16) * 0.4)(F) * 0.01),
            "w2": _w((lambda *s: torch.randn(*s, dtype=torch.bfloat16) * 0.4)(F, D) * (F ** -0.5)),
            "b2": _w((lambda *s: torch.randn(*s, dtype=torch.bfloat16) * 0.4)(D) * 0.01),
        }
    _B_in = _B_setup()

    return {
        "A_x": _A_in["x"],
        "A_k_cache": _A_in["k_cache"],
        "A_v_cache": _A_in["v_cache"],
        "A_wq": _A_in["wq"],
        "A_wo": _A_in["wo"],
        "B_x": _B_in["x"],
        "B_w1": _B_in["w1"],
        "B_b1": _B_in["b1"],
        "B_w2": _B_in["w2"],
        "B_b2": _B_in["b2"],
    }

def reference(A_x, A_k_cache, A_v_cache, A_wq, A_wo, B_x, B_w1, B_b1, B_w2, B_b2):
    def _A_ref(x, k_cache, v_cache, wq, wo):
        B, Sq, D = x.shape
        H, Dh = 32, 128
        q = (lambda x, *d: x.permute(*d))((x @ wq).reshape(B, Sq, H, Dh), 0, 2, 1, 3)
        scores = torch.einsum("bhqd,bhkd->bhqk", q, k_cache) * (Dh ** -0.5)
        p = _softmax(scores, axis=-1)
        o = torch.einsum("bhqk,bhkd->bhqd", p, v_cache)
        return (lambda x, *d: x.permute(*d))(o, 0, 2, 1, 3).reshape(B, Sq, H * Dh) @ wo

    def _B_ref(x, w1, b1, w2, b2):
        h = torch.einsum("bsd,df->bsf", x, w1) + b1
        h = _gelu(h)
        return torch.einsum("bsf,fd->bsd", h, w2) + b2

    x = A_x
    x = x + _A_ref(_layernorm(x, axis=-1), A_k_cache, A_v_cache, A_wq, A_wo)
    x = x + _B_ref(_layernorm(x, axis=-1), B_w1, B_b1, B_w2, B_b2)
    x = x + _A_ref(_layernorm(x, axis=-1), A_k_cache, A_v_cache, A_wq, A_wo)
    x = x + _B_ref(_layernorm(x, axis=-1), B_w1, B_b1, B_w2, B_b2)
    return x
```

```cpp
#include <hip/hip_runtime.h>
#include <stdint.h>

typedef unsigned short u16;
typedef unsigned v4u __attribute__((ext_vector_type(4)));
typedef unsigned v2u __attribute__((ext_vector_type(2)));
typedef float v4f __attribute__((ext_vector_type(4)));
typedef float v2f __attribute__((ext_vector_type(2)));
typedef __bf16 bf8 __attribute__((ext_vector_type(8)));
#define LAS __attribute__((address_space(3)))

constexpr int DM = 512, NH = 32, SK = 4096, DH = 128, FF = 1376;
constexpr int NWG = 256, NT = 512;
constexpr int NMLP = 43;
constexpr int NROWS = 16 + NMLP;
constexpr unsigned SENT = 0xFFFFFFFFu;
constexpr int NREP = 2;
constexpr int WS_EPOCH = 0;
constexpr int WS_FLAGS = 1024;
constexpr int WS_D0 = 4096, D0_SLOT = 1024;
constexpr int WS_DC = WS_D0 + NWG * D0_SLOT, DC_SLOT = 1024;
constexpr int WS_D1 = WS_DC + NWG * DC_SLOT, D1_SLOT = 640, D1_REP = NWG * D1_SLOT;
constexpr int WS_A = WS_D1 + NREP * D1_REP, A_SLOT = 384, A_REP = NWG * A_SLOT, A_LAYER = NREP * A_REP;
constexpr int WS_R = WS_A + 2 * A_LAYER, R_ROW = 1024, R_REP = 64 * R_ROW, R_LAYER = NREP * R_REP;
constexpr int WS_END = WS_R + 2 * R_LAYER;
constexpr int L_QP = 0;
constexpr int L_APART = 0;
constexpr int L_QFIN = 8320;
constexpr int L_CNT = 8576;
constexpr int L_XS = 8704;
constexpr int L_RED = 10752;
constexpr int L_HB = 12800;
constexpr int L_XB = 13312;
constexpr int L_CSUM = 15872;
constexpr int L_VT = 16384;
constexpr int L_WQ = L_VT + 131072;
constexpr int LDS_BYTES = L_WQ + 16384;

constexpr float C_SCALE = 0.08838834764831845f * 1.4426950408889634f;
constexpr unsigned SPIN_CAP = 1u << 16;
constexpr unsigned ONES2 = 0x3F803F80u;
constexpr int QW = 0;

__device__ __forceinline__ float bf_lo(unsigned u) { return __uint_as_float(u << 16); }
__device__ __forceinline__ float bf_hi(unsigned u) { return __uint_as_float(u & 0xffff0000u); }
__device__ __forceinline__ unsigned cvtpk(float lo, float hi) {
  typedef __bf16 bf2 __attribute__((ext_vector_type(2)));
  return __builtin_bit_cast(unsigned, __builtin_convertvector(v2f{lo, hi}, bf2));
}
__device__ __forceinline__ unsigned nosent(unsigned u) { return min(u, 0xFFFFFFFEu); }
__device__ __forceinline__ unsigned fbits(float f) { return nosent(__float_as_uint(f)); }
template <int CTRL> __device__ __forceinline__ unsigned dppu(unsigned x) { return (unsigned)__builtin_amdgcn_mov_dpp((int)x, CTRL, 0xf, 0xf, true); }
template <int CTRL> __device__ __forceinline__ float dppf(float x) { return __uint_as_float(dppu<CTRL>(__float_as_uint(x))); }
constexpr int XOR1 = 0xB1, XOR2 = 0x4E, HMIR = 0x141, ROR4 = 0x124, ROR8 = 0x128, NBC = 0x150, SHL4 = 0x104, SHL8 = 0x108;
__device__ __forceinline__ float x16_add(float x) { auto s = __builtin_amdgcn_permlane16_swap(__float_as_uint(x), __float_as_uint(x), false, false); return __uint_as_float(s[0]) + __uint_as_float(s[1]); }
__device__ __forceinline__ float x32_add(float x) { auto s = __builtin_amdgcn_permlane32_swap(__float_as_uint(x), __float_as_uint(x), false, false); return __uint_as_float(s[0]) + __uint_as_float(s[1]); }
__device__ __forceinline__ float vmax(float a, float b) { float r; asm("v_max_f32 %0, %1, %2" : "=v"(r) : "v"(a), "v"(b)); return r; }
__device__ __forceinline__ float vmax3(float a, float b, float c) { float r; asm("v_max3_f32 %0, %1, %2, %3" : "=v"(r) : "v"(a), "v"(b), "v"(c)); return r; }
#define DPPMAX(x, CTL) asm("s_nop 1\n\tv_max_f32_dpp %0, %0, %0 " CTL " row_mask:0xf bank_mask:0xf" : "+v"(x))
#define PIN(x) asm volatile("" : "+v"(x))
__device__ __forceinline__ float x16_max(float x) { float y; asm("v_mov_b32 %1, %0\n\ts_nop 1\n\tv_permlane16_swap_b32 %0, %1\n\ts_nop 0\n\tv_max_f32 %0, %0, %1" : "+v"(x), "=&v"(y)); return x; }
__device__ __forceinline__ float x32_max(float x) { float y; asm("v_mov_b32 %1, %0\n\ts_nop 1\n\tv_permlane32_swap_b32 %0, %1\n\ts_nop 0\n\tv_max_f32 %0, %0, %1" : "+v"(x), "=&v"(y)); return x; }
__device__ __forceinline__ v4f mfma(v4u a, v4u b, v4f c) {
  return __builtin_amdgcn_mfma_f32_16x16x32_bf16(__builtin_bit_cast(bf8, a), __builtin_bit_cast(bf8, b), c, 0, 0, 0);
}
__device__ __forceinline__ v2u trread(LAS unsigned char* L, int off) {
  typedef short v4i16_t __attribute__((ext_vector_type(4)));
  return __builtin_bit_cast(v2u, __builtin_amdgcn_ds_read_tr16_b64_v4i16((LAS v4i16_t*)(L + off)));
}
#define SCHED() __builtin_amdgcn_sched_barrier(0)
#define BAR() asm volatile("s_waitcnt lgkmcnt(0)\n\ts_barrier" ::: "memory")
#define LDSWAIT() asm volatile("s_waitcnt lgkmcnt(0)" ::: "memory")
__device__ __forceinline__ int fresh_lane() { int l; asm volatile("v_mbcnt_lo_u32_b32 %0, -1, 0\n\tv_mbcnt_hi_u32_b32 %0, -1, %0" : "=v"(l)); return l; }
#define LANES() const int lane = fresh_lane(), l15 = lane & 15, g = lane >> 4; (void)l15; (void)g
#define MASK8() unsigned m8[4]; _Pragma("unroll") for (int d = 0; d < 4; ++d) m8[d] = (((l15 & 7) >> 1) == d) ? ((l15 & 1) ? 0xffff0000u : 0x0000ffffu) : 0u
#define IDENT() v4u ident; _Pragma("unroll") for (int d = 0; d < 4; ++d) ident[d] = ((l15 >> 1) == d && l15 < 8) ? ((l15 & 1) ? 0x3F800000u : 0x00003F80u) : 0u
#define GLD(dst, voff, sbase, imm) asm volatile("global_load_dwordx4 %0, %1, %2 offset:%3" : "=v"(dst) : "v"(voff), "s"(sbase), "n"(imm))
#define GLD_NT(dst, voff, sbase, imm) asm volatile("global_load_dwordx4 %0, %1, %2 offset:%3 nt" : "=v"(dst) : "v"(voff), "s"(sbase), "n"(imm))
#define GLD_W GLD_NT
#define GLD_KV GLD_NT
#define AUX_SC1 16
typedef __amdgpu_buffer_rsrc_t rsrc_t;
__device__ __forceinline__ v4u ld4(rsrc_t rs, int off) { return __builtin_amdgcn_raw_buffer_load_b128(rs, off, 0, AUX_SC1); }
__device__ __forceinline__ v2u ld2(rsrc_t rs, int off) { return __builtin_amdgcn_raw_buffer_load_b64(rs, off, 0, AUX_SC1); }
__device__ __forceinline__ void st4(rsrc_t rs, int off, v4u v) { __builtin_amdgcn_raw_buffer_store_b128(v, rs, off, 0, AUX_SC1); }
__device__ __forceinline__ void st1(rsrc_t rs, int off, unsigned v) { __builtin_amdgcn_raw_buffer_store_b32(v, rs, off, 0, AUX_SC1); }
__device__ __forceinline__ unsigned umax4(unsigned m, v4u v) { return max(max(max(m, v.x), max(v.y, v.z)), v.w); }
__device__ __forceinline__ unsigned umax2(unsigned m, v2u v) { return max(max(m, v.x), v.y); }
constexpr int OOB = 0x7ff00000;
__device__ __forceinline__ void delay(int n) { for (int i = 0; i < n; ++i) __builtin_amdgcn_s_sleep(1); }
template <int N>
__device__ __forceinline__ void gather4(rsrc_t rs, const int (&off)[N], int rep, v4u (&v)[N]) {
  unsigned spins = 0;
  for (int ro = 0; spins < SPIN_CAP; ++spins) {
    asm volatile("" ::: "memory");
#pragma unroll
    for (int i = 0; i < N; ++i) v[i] = ld4(rs, off[i] + ro);
    unsigned m = 0;
#pragma unroll
    for (int i = 0; i < N; ++i) m = umax4(m, v[i]);
    if (!__any(m == SENT)) break;
    if (spins < NREP - 1) ro += rep;
  }
}
constexpr int kDelay[12] = {0, 12, 0, 55, 10, 35, 10, 8, 8, 80, 80, 45};

__global__ void __launch_bounds__(NT, 2)
mk_kernel(const u16* __restrict__ x, const u16* __restrict__ kc, const u16* __restrict__ vc, const u16* __restrict__ wq,
          const u16* __restrict__ wo, const u16* __restrict__ w1, const u16* __restrict__ b1, const u16* __restrict__ w2,
          const u16* __restrict__ b2, u16* __restrict__ out, unsigned char* __restrict__ ws) {
  extern __shared__ __attribute__((aligned(16))) unsigned char smem[];
  LAS unsigned char* L = (LAS unsigned char*)smem;
  const int w = __builtin_amdgcn_readfirstlane(threadIdx.x >> 6);
  const int b = blockIdx.x, xg = b & 7, idx = b >> 3;
  const int hh = idx >> 3, h = 4 * xg + hh, j = idx & 7, pidx = xg * 32 + idx;
  const bool tileRole = (hh & 1) == 0;
  const int prow = 2 * xg + (hh >> 1);
  const int mi = xg + 8 * (idx - 8);
  const bool mlpRole = hh == 1 && mi < NMLP;
  const bool finRole = hh == 3 && xg == j;
  const rsrc_t rs = __builtin_amdgcn_make_buffer_rsrc(ws, 0, WS_END, 0x00020000);

  v4u vr[16], kr[16], wr[8];
  unsigned tag = 0;
  v4f ctot = {0.f, 0.f, 0.f, 0.f};
  float c1tot = 0.f;
  float resid0 = 0.f, resid1 = 0.f;

  {
    LANES();
    const u16* sk = kc + ((size_t)h * SK + j * 512 + w * 4) * DH;
    const u16* sv = vc + ((size_t)h * SK + j * 512 + w * 4) * DH;
    constexpr int BST = 4096;
    const int vok = (((lane >> 2) & 3) * DH + ((lane & 3) + 4 * g) * 8) * 2, vov = lane * 16;
#define LDK(i) GLD_KV(kr[i], vok, sk + (i) * BST, 0)
#define LDV(i) GLD_KV(vr[i], vov, sv + (i) * BST, 0)
    unsigned epoch, xk, warm; v4u xall, wqr[2];
    {
      const u16* sb = wq + (size_t)(64 * j + 8 * w) * (NH * DH) + h * DH;
      const int vo = g * (NH * DH * 2) + l15 * 16;
      GLD_W(wqr[0], vo, sb, 0);
      GLD_W(wqr[1], vo, sb + 4 * (NH * DH), 0);
      asm volatile("global_load_ushort %0, %1, %2" : "=v"(xk) : "v"((64 * j + 8 * w + 4 * (l15 & 1) + g) * 2), "s"(x));
      GLD(xall, lane * 16, x, 0);
      asm volatile("global_load_dword %0, %1, %2 sc1" : "=v"(epoch) : "v"(WS_EPOCH), "s"(ws));
      warm = 0;
      const unsigned char* p = (const unsigned char*)x;
      if (lane < 2) { if (tileRole) p = (const unsigned char*)(wo + (size_t)(h * DH + 128 * lane) * DM + 64 * j); }
      else if (lane < 4) { if (mlpRole) p = (const unsigned char*)(w1 + (size_t)(lane - 2) * 511 * FF + 32 * mi); }
      else if (lane == 4) { if (mlpRole) p = (const unsigned char*)(w2 + (size_t)(32 * mi) * DM); }
      else if (lane == 5) p = (const unsigned char*)out;
      else if (lane == 6) p = ws + WS_R;
      asm volatile("global_load_dword %0, %1, off" : "=v"(warm) : "v"(p));
    }
    if (w == 0 && lane < 2) *(LAS unsigned*)(L + L_CNT + 4 * lane) = 0u;
    BAR();
    auto partial_q = [&]() {
      const unsigned xp = xk | (xk << 16);
      MASK8();
      v4f acc = {0.f, 0.f, 0.f, 0.f};
#pragma unroll
      for (int bb = 0; bb < 2; ++bb) {
        unsigned pd = bb ? dppu<NBC + 1>(xp) : dppu<NBC + 0>(xp);
        asm volatile("" : "+v"(pd));
        const unsigned pb = l15 < 8 ? pd : ONES2;
        v4u aa;
#pragma unroll
        for (int d = 0; d < 4; ++d) aa[d] = pb & m8[d];
        acc = mfma(aa, wqr[bb], acc);
        *(LAS v4u*)(L + L_WQ + (2 * w + bb) * 1024 + lane * 16) = wqr[bb];
      }
      *(LAS v4f*)(L + L_QP + ((w * 2 + (g >> 1)) * 128 + 8 * l15 + 4 * (g & 1)) * 4) = acc;
      LDSWAIT();
      if (lane == 0) __hip_atomic_fetch_add((LAS unsigned*)(L + L_CNT), 1u, __ATOMIC_RELAXED, __HIP_MEMORY_SCOPE_WORKGROUP);
    };
    if (w < 4) {
#pragma unroll
      for (int i = 0; i < 16; ++i) LDK(i);
      asm volatile("s_waitcnt vmcnt(16)");
    } else asm volatile("s_waitcnt vmcnt(0)");
    asm volatile("; WAITREGS %0 %1 %2 %3 %4 %5" : "+v"(epoch), "+v"(xk), "+v"(xall), "+v"(wqr[0]), "+v"(wqr[1]), "+v"(warm));
    tag = __builtin_amdgcn_readfirstlane(epoch) * 0x9E3779B5u + 0x7F4A7C15u;
    partial_q();
    if (w >= 4) {
#pragma unroll
      for (int i = 0; i < 16; ++i) LDK(i);
    }
    SCHED();
#pragma unroll
    for (int i = 0; i < 16; ++i) LDV(i);
    if (w >= 1 && w <= 3) {
      const v4u s4 = {SENT, SENT, SENT, SENT};
      if (w == 1) { if (lane < 24 * NREP) st4(rs, WS_A + (lane / 24) * A_REP + pidx * A_SLOT + (lane % 24) * 16, s4); }
      else if (tileRole) { if (lane < 8) st4(rs, WS_R + (w - 2) * R_REP + prow * R_ROW + j * 128 + lane * 16, s4); }
      else if (mlpRole) st4(rs, WS_R + (w - 2) * R_REP + (16 + mi) * R_ROW + lane * 16, s4);
      if (w == 1) { if (lane < 24 * NREP) st4(rs, WS_A + A_LAYER + (lane / 24) * A_REP + pidx * A_SLOT + (lane % 24) * 16, s4); }
      else {
        if (lane < 40) st4(rs, WS_D1 + (w - 2) * D1_REP + pidx * D1_SLOT + lane * 16, s4);
        if (tileRole) { if (lane < 8) st4(rs, WS_R + R_LAYER + (w - 2) * R_REP + prow * R_ROW + j * 128 + lane * 16, s4); }
        else if (mlpRole) st4(rs, WS_R + R_LAYER + (w - 2) * R_REP + (16 + mi) * R_ROW + lane * 16, s4);
      }
      asm volatile("s_waitcnt vmcnt(0)" ::: "memory");
      if (lane == 0) __hip_atomic_fetch_add((LAS unsigned*)(L + L_CNT + 4), 1u, __ATOMIC_RELAXED, __HIP_MEMORY_SCOPE_WORKGROUP);
    }
    if (w == QW) {
      float s1 = 0.f, s2 = 0.f;
#pragma unroll
      for (int d = 0; d < 4; ++d) { const float a = bf_lo(xall[d]), c = bf_hi(xall[d]); s1 += a + c; s2 += a * a + c * c; }
      s1 += dppf<XOR1>(s1); s2 += dppf<XOR1>(s2); s1 += dppf<XOR2>(s1); s2 += dppf<XOR2>(s2);
      s1 += dppf<ROR4>(s1); s2 += dppf<ROR4>(s2); s1 += dppf<ROR8>(s1); s2 += dppf<ROR8>(s2);
      s1 = x32_add(x16_add(s1)); s2 = x32_add(x16_add(s2));
      const float mu = s1 * (1.f / DM), rstd = __builtin_amdgcn_rsqf(fmaxf(s2 * (1.f / DM) - mu * mu, 0.f) + 1e-5f);
      for (unsigned spins = 0; spins < (1u << 22); ++spins) {
        if (__hip_atomic_load((LAS unsigned*)(L + L_CNT), __ATOMIC_RELAXED, __HIP_MEMORY_SCOPE_WORKGROUP) >= 8u) break;
        __builtin_amdgcn_s_sleep(1);
      }
      asm volatile("" ::: "memory");
      v4f qv[8];
#pragma unroll
      for (int ww = 0; ww < 8; ++ww) qv[ww] = *(LAS v4f*)(L + L_QP + ((ww * 2 + (lane >> 5)) * 128 + 4 * (lane & 31)) * 4);
      const v4f qs = ((qv[0] + qv[1]) + (qv[2] + qv[3])) + ((qv[4] + qv[5]) + (qv[6] + qv[7]));
      v4f qn;
#pragma unroll
      for (int d = 0; d < 4; ++d) {
        auto sw = __builtin_amdgcn_permlane32_swap(__float_as_uint(qs[d]), __float_as_uint(qs[d]), false, false);
        qn[d] = rstd * (__uint_as_float(sw[0]) - mu * __uint_as_float(sw[1]));
      }
      if (lane >= 32) *(LAS v4f*)(L + L_CSUM + (lane & 31) * 16) = qs;
      if (lane < 32) {
        st4(rs, WS_D0 + pidx * D0_SLOT + lane * 32, v4u{__float_as_uint(qn[0]), tag, __float_as_uint(qn[1]), tag});
        st4(rs, WS_D0 + pidx * D0_SLOT + lane * 32 + 16, v4u{__float_as_uint(qn[2]), tag, __float_as_uint(qn[3]), tag});
      }
      delay(kDelay[11]);
      const int db = WS_D0 + (xg * 32 + (idx & ~7)) * D0_SLOT + lane * 16;
      v4u pq[8];
      unsigned spins = 0;
      for (; spins < SPIN_CAP; ++spins) {
        asm volatile("" ::: "memory");
#pragma unroll
        for (int p = 0; p < 8; ++p) pq[p] = ld4(rs, db + p * D0_SLOT);
        bool ok = true;
#pragma unroll
        for (int p = 0; p < 8; ++p) ok = ok && pq[p].y == tag && pq[p].w == tag;
        if (__all(ok)) break;
        delay(kDelay[6]);
      }
      float q0 = 0.f, q1 = 0.f;
#pragma unroll
      for (int p = 0; p < 8; ++p) { q0 += __uint_as_float(pq[p].x); q1 += __uint_as_float(pq[p].z); }
      *(LAS unsigned*)(L + L_QFIN + lane * 4) = cvtpk(q0, q1);
      for (unsigned sp = 0; sp < (1u << 22); ++sp) {
        if (__hip_atomic_load((LAS unsigned*)(L + L_CNT + 4), __ATOMIC_RELAXED, __HIP_MEMORY_SCOPE_WORKGROUP) >= 3u) break;
        __builtin_amdgcn_s_sleep(1);
      }
      if (lane == 0) st1(rs, WS_FLAGS + 4 * b, tag);
    }
    BAR();
  }

#pragma unroll
  for (int LY = 0; LY < 2; ++LY) {
    {
      LANES();
      if (LY == 1 && w < 4) __builtin_amdgcn_s_setprio(1);
      v4u qb[4];
      {
        const v4u qbase = *(LAS v4u*)(L + L_QFIN + 16 * ((l15 & 3) + 4 * g));
#pragma unroll
        for (int t = 0; t < 4; ++t)
#pragma unroll
          for (int d = 0; d < 4; ++d) qb[t][d] = ((l15 >> 2) == t) ? qbase[d] : 0u;
      }
      v4f a[4];
      if (LY == 0)
        asm volatile("s_waitcnt vmcnt(16) ; WAITREGS %0 %1 %2 %3 %4 %5 %6 %7 %8 %9 %10 %11 %12 %13 %14 %15" : "+v"(kr[0]), "+v"(kr[1]), "+v"(kr[2]), "+v"(kr[3]), "+v"(kr[4]), "+v"(kr[5]), "+v"(kr[6]), "+v"(kr[7]),
                     "+v"(kr[8]), "+v"(kr[9]), "+v"(kr[10]), "+v"(kr[11]), "+v"(kr[12]), "+v"(kr[13]), "+v"(kr[14]), "+v"(kr[15]));
#pragma unroll
      for (int u = 0; u < 4; ++u) a[u] = mfma(kr[4 * u], qb[0], v4f{0.f, 0.f, 0.f, 0.f});
#pragma unroll
      for (int t = 1; t < 4; ++t)
#pragma unroll
        for (int u = 0; u < 4; ++u) a[u] = mfma(kr[4 * u + t], qb[t], a[u]);
      float sc[4];
#pragma unroll
      for (int u = 0; u < 4; ++u) {
        const int s = lane & 3;
        float v = (s & 2) ? ((s & 1) ? a[u][3] : a[u][2]) : ((s & 1) ? a[u][1] : a[u][0]);
        v += dppf<XOR1>(v); v += dppf<XOR2>(v);
        sc[u] = v;
      }
      float m = vmax(vmax3(sc[0], sc[1], sc[2]), sc[3]);
      DPPMAX(m, "row_ror:4"); DPPMAX(m, "row_ror:8"); m = x32_max(x16_max(m));
      m *= C_SCALE;
      float p[4];
#pragma unroll
      for (int u = 0; u < 4; ++u) p[u] = __builtin_amdgcn_exp2f(sc[u] * C_SCALE - m);
      float ls;
      if (LY == 0) {
        ls = (p[0] + p[1]) + (p[2] + p[3]);
        ls += dppf<ROR4>(ls); ls += dppf<ROR8>(ls); ls = x32_add(x16_add(ls));
        if (lane == 0) *(LAS v2f*)(L + L_APART + (w * 132 + 128) * 4) = v2f{m, ls};
        unsigned pp[4];
#pragma unroll
        for (int u = 0; u < 4; ++u) pp[u] = cvtpk(p[u], p[u]);
        unsigned md[4];
#pragma unroll
        for (int d = 0; d < 4; ++d) md[d] = ((l15 >> 1) == d && l15 < 8) ? ((l15 & 1) ? 0xffff0000u : 0x0000ffffu) : 0u;
        const u16* s0 = tileRole ? wo + (size_t)(h * DH + 32 * w) * DM + 64 * j : mlpRole ? w2 + (size_t)(32 * mi) * DM + 64 * w : wo;
        const int vo0 = (tileRole || mlpRole) ? (lane >> 3) * (DM * 2) + (lane & 7) * 16 : 0;
        const int st0 = (tileRole || mlpRole) ? 8 * DM : 0;
        v4f o0 = {0.f, 0.f, 0.f, 0.f}, o1 = {0.f, 0.f, 0.f, 0.f};
#pragma unroll
        for (int i = 0; i < 16; ++i) {
          v4u aa;
          asm volatile("s_waitcnt vmcnt(%2) ; WAITREGS %0 %1" : "+v"(vr[i]), "+v"(pp[i >> 2]) : "n"(15 - i + (i < 4 ? i : 4)));
          if (i < 4) GLD_W(wr[i], vo0, s0 + i * st0, 0);
          const unsigned pb = (i & 3) == 0 ? dppu<NBC + 0>(pp[i >> 2]) : (i & 3) == 1 ? dppu<NBC + 4>(pp[i >> 2]) : (i & 3) == 2 ? dppu<NBC + 8>(pp[i >> 2]) : dppu<NBC + 12>(pp[i >> 2]);
#pragma unroll
          for (int d = 0; d < 4; ++d) aa[d] = pb & md[d];
          if (i & 1) o1 = mfma(aa, vr[i], o1); else o0 = mfma(aa, vr[i], o0);
        }
        o0 += o1;
        PIN(o0);
        if (lane < 32) *(LAS v4f*)(L + L_APART + (w * 132 + 8 * l15 + 4 * g) * 4) = o0;
      } else {
        v4u pa[2];
#pragma unroll
        for (int u = 0; u < 4; ++u) {
          const unsigned xu = cvtpk(p[u], dppf<SHL4>(p[u]));
          pa[u >> 1][2 * (u & 1)] = xu; pa[u >> 1][2 * (u & 1) + 1] = dppu<SHL8>(xu);
        }
        v4f o[8];
#pragma unroll
        for (int cg = 0; cg < 8; ++cg) o[cg] = mfma(pa[0], vr[2 * cg], v4f{0.f, 0.f, 0.f, 0.f});
        SCHED();
        ls = (p[0] + p[1]) + (p[2] + p[3]);
        ls += dppf<ROR4>(ls); ls += dppf<ROR8>(ls); ls = x32_add(x16_add(ls));
        if (lane == 0) *(LAS v2f*)(L + L_APART + (w * 132 + 128) * 4) = v2f{m, ls};
        SCHED();
#pragma unroll
        for (int cg = 0; cg < 8; ++cg) o[cg] = mfma(pa[1], vr[2 * cg + 1], o[cg]);
        if (lane < 16) {
#pragma unroll
          for (int cg = 0; cg < 8; ++cg) *(LAS float*)(L + L_APART + (w * 132 + 16 * cg + l15) * 4) = o[cg][0];
        }
      }
      __builtin_amdgcn_s_setprio(0);
      BAR();
      if (w == 0) {
        int ao = WS_A + LY * A_LAYER + (lane >> 5) * A_REP + pidx * A_SLOT + ((lane & 31) == 1 ? 256 : (lane & 31) * 8);
        PIN(ao);
        v2f ml[8]; v4f ov[8];
#pragma unroll
        for (int ww = 0; ww < 8; ++ww) ml[ww] = *(LAS v2f*)(L + L_APART + (ww * 132 + 128) * 4);
#pragma unroll
        for (int ww = 0; ww < 8; ++ww) ov[ww] = *(LAS v4f*)(L + L_APART + (ww * 132 + 4 * (lane & 31)) * 4);
        const float M = vmax(vmax3(ml[0][0], ml[1][0], ml[2][0]), vmax3(vmax3(ml[3][0], ml[4][0], ml[5][0]), ml[6][0], ml[7][0]));
        float f[8];
#pragma unroll
        for (int ww = 0; ww < 8; ++ww) f[ww] = __builtin_amdgcn_exp2f(ml[ww][0] - M);
        const v4f o = ((f[0] * ov[0] + f[1] * ov[1]) + (f[2] * ov[2] + f[3] * ov[3])) + ((f[4] * ov[4] + f[5] * ov[5]) + (f[6] * ov[6] + f[7] * ov[7]));
        float La = f[0] * ml[0][1], Lb = f[4] * ml[4][1];
#pragma unroll
        for (int ww = 1; ww < 4; ++ww) { La = __builtin_fmaf(f[ww], ml[ww][1], La); Lb = __builtin_fmaf(f[4 + ww], ml[4 + ww][1], Lb); }
        const unsigned p0 = cvtpk(o[0], o[1]), p1 = cvtpk(o[2], o[3]);
        const unsigned n0 = dppu<XOR1>(p0), n1 = dppu<XOR1>(p1);
        const bool tail = (lane & 31) == 1;
        const v4u pv = {nosent(tail ? __float_as_uint(M) : p0), nosent(tail ? __float_as_uint(La + Lb) : p1), nosent(tail ? 0u : n0), nosent(tail ? 0u : n1)};
        if ((lane & 1) == 0 || tail) st4(rs, ao, pv);
      }
    }
    BAR();
    auto transpose_v = [&]() {
      LANES();
      LAS unsigned char* S = L + L_VT + w * 16384;
#pragma unroll
      for (int i = 0; i < 16; ++i) *(LAS v4u*)(S + 1024 * i + 256 * g + 16 * (l15 ^ ((g << 2) | (i & 3)))) = vr[i];
      const int q = l15 >> 2, p = l15 & 3;
      const int a0 = 256 * (4 * q + g) + 16 * ((p >> 1) ^ ((g << 2) | q)) + 8 * (p & 1);
#pragma unroll
      for (int cg = 0; cg < 8; ++cg)
#pragma unroll
        for (int s = 0; s < 2; ++s) {
          const v2u t0 = trread(S, (a0 ^ (32 * cg)) + 8192 * s), t1 = trread(S, (a0 ^ (32 * cg)) + 8192 * s + 4096);
          vr[2 * cg + s] = v4u{t0.x, t0.y, t1.x, t1.y};
        }
      LDSWAIT();
    };
    if (LY == 0) {
      LANES();
      LAS unsigned char* S = L + L_VT + w * 16384;
      const int q = l15 >> 2, p = l15 & 3;
      if (w == 7) {
        for (unsigned spins = 0; spins < SPIN_CAP; ++spins) {
          asm volatile("" ::: "memory");
          const v4u f2 = ld4(rs, WS_FLAGS + 16 * lane);
          if (__all(f2.x == tag && f2.y == tag && f2.z == tag && f2.w == tag)) break;
          __builtin_amdgcn_s_sleep(2);
        }
        if (b == 0 && lane == 0) st1(rs, WS_EPOCH, tag);
      }
      if (w == 0) {
        if (lane < 32) {
          const v4f csum = *(LAS v4f*)(L + L_CSUM + lane * 16);
          st4(rs, WS_DC + pidx * DC_SLOT + lane * 32, v4u{__float_as_uint(csum[0]), tag, __float_as_uint(csum[1]), tag});
          st4(rs, WS_DC + pidx * DC_SLOT + lane * 32 + 16, v4u{__float_as_uint(csum[2]), tag, __float_as_uint(csum[3]), tag});
        }
        asm volatile("s_waitcnt vmcnt(3)");
      } else asm volatile("s_waitcnt vmcnt(0)");
      asm volatile("; WAITREGS %0 %1 %2 %3" : "+v"(wr[0]), "+v"(wr[1]), "+v"(wr[2]), "+v"(wr[3]));
      if (tileRole) {
#pragma unroll
        for (int i = 0; i < 4; ++i) *(LAS v4u*)(S + (8 * i + (lane >> 3)) * 128 + (lane & 7) * 16) = wr[i];
#pragma unroll
        for (int cg = 0; cg < 4; ++cg) {
          const int a0 = (8 * g + q) * 128 + cg * 32 + p * 8;
          const v2u t0 = trread(S, a0), t1 = trread(S, a0 + 4 * 128);
          wr[4 + cg] = v4u{t0.x, t0.y, t1.x, t1.y};
        }
        LDSWAIT();
        if (prow == 0) { const unsigned xv = *(const unsigned*)(x + 64 * j + 2 * (lane & 31)); resid0 = bf_lo(xv); resid1 = bf_hi(xv); }
      } else if (mlpRole) {
        {
          const u16* s1 = w1 + (size_t)(64 * w) * FF + 32 * mi;
          const int vo1 = (lane >> 2) * (FF * 2) + (lane & 3) * 16;
#pragma unroll
          for (int i = 0; i < 4; ++i) GLD_W(wr[4 + i], vo1, s1 + i * 16 * FF, 0);
        }
        v4u w2f[4];
#pragma unroll
        for (int i = 0; i < 4; ++i) *(LAS v4u*)(S + (8 * i + (lane >> 3)) * 128 + (lane & 7) * 16) = wr[i];
#pragma unroll
        for (int cg = 0; cg < 4; ++cg) {
          const int a0 = (8 * g + q) * 128 + cg * 32 + p * 8;
          const v2u t0 = trread(S, a0), t1 = trread(S, a0 + 4 * 128);
          w2f[cg] = v4u{t0.x, t0.y, t1.x, t1.y};
        }
        LDSWAIT();
        transpose_v();
        asm volatile("s_waitcnt vmcnt(0) ; WAITREGS %0 %1 %2 %3" : "+v"(wr[4]), "+v"(wr[5]), "+v"(wr[6]), "+v"(wr[7]));
#pragma unroll
        for (int i = 0; i < 4; ++i) *(LAS v4u*)(S + (16 * i + (lane >> 2)) * 64 + (lane & 3) * 16) = wr[4 + i];
#pragma unroll
        for (int s = 0; s < 2; ++s)
#pragma unroll
          for (int cg = 0; cg < 2; ++cg) {
            const int a0 = (32 * s + 8 * g + q) * 64 + cg * 32 + p * 8;
            const v2u t0 = trread(S, a0), t1 = trread(S, a0 + 4 * 64);
            wr[s * 2 + cg] = v4u{t0.x, t0.y, t1.x, t1.y};
          }
#pragma unroll
        for (int cg = 0; cg < 4; ++cg) wr[4 + cg] = w2f[cg];
        const v4u ones = {ONES2, ONES2, ONES2, ONES2};
#pragma unroll
        for (int cg = 0; cg < 2; ++cg) {
          v4f c = mfma(ones, wr[cg], v4f{0.f, 0.f, 0.f, 0.f}); c = mfma(ones, wr[2 + cg], c);
          if (lane < 16) *(LAS float*)(L + L_RED + (w * 32 + cg * 16 + l15) * 4) = c[0];
        }
        BAR();
        float cv[8];
#pragma unroll
        for (int ww = 0; ww < 8; ++ww) cv[ww] = *(LAS float*)(L + L_RED + (ww * 32 + (lane & 31)) * 4);
        c1tot = ((cv[0] + cv[1]) + (cv[2] + cv[3])) + ((cv[4] + cv[5]) + (cv[6] + cv[7]));
      } else transpose_v();
      BAR();
    }
    if (tileRole) {
      LANES();
      const int sb = WS_A + LY * A_LAYER + (xg * 32 + 8 * (hh + (w >> 2))) * A_SLOT;
      const int o0 = sb + (lane & 7) * A_SLOT + (32 * (w & 3) + 8 * g) * 2;
      const int om = sb + 4 * (g & 1) * A_SLOT + 256;
      int lr = L_RED + (w * 64 + lane) * 4, lq = L_RED + 2 * (lane & 31) * 4;
      int po = WS_R + LY * R_LAYER + (lane >> 5) * R_REP + prow * R_ROW + j * 128 + (lane & 31) * 4;
      unsigned glo = g < 2 ? 1u : 0u, godd = g & 1;
      PIN(lr); PIN(lq); PIN(po); PIN(glo); PIN(godd);
      v4u a; v2u ml[4];
      delay(kDelay[LY == 0 ? 0 : 1]);
      unsigned spins = 0;
      for (int ro = 0; spins < SPIN_CAP; ++spins) {
        asm volatile("" ::: "memory");
        a = ld4(rs, o0 + ro);
#pragma unroll
        for (int r = 0; r < 4; ++r) ml[r] = ld2(rs, om + ro + r * A_SLOT);
        const unsigned mx = umax2(umax2(umax2(umax2(umax4(0u, a), ml[0]), ml[1]), ml[2]), ml[3]);
        if (!__any(mx == SENT)) break;
        if (spins < NREP - 1) ro += A_REP; else delay(kDelay[8]);
      }
      v4f acc[4];
#pragma unroll
      for (int cg = 0; cg < 4; ++cg) acc[cg] = mfma(a, wr[4 + cg], v4f{0.f, 0.f, 0.f, 0.f});
      SCHED();
      float M = vmax(vmax3(__uint_as_float(ml[0].x), __uint_as_float(ml[1].x), __uint_as_float(ml[2].x)), __uint_as_float(ml[3].x));
      M = x16_max(M);
      v2f c01, c23;
      c01[0] = __builtin_amdgcn_exp2f(__uint_as_float(ml[0].x) - M); c01[1] = __builtin_amdgcn_exp2f(__uint_as_float(ml[1].x) - M);
      c23[0] = __builtin_amdgcn_exp2f(__uint_as_float(ml[2].x) - M); c23[1] = __builtin_amdgcn_exp2f(__uint_as_float(ml[3].x) - M);
      float t[4];
#pragma unroll
      for (int cg = 0; cg < 4; ++cg) { const v2f u = c01 * v2f{acc[cg][0], acc[cg][1]} + c23 * v2f{acc[cg][2], acc[cg][3]}; t[cg] = u[0] + u[1]; }
      const float Ls = __builtin_fmaf(c01[0], __uint_as_float(ml[0].y), c01[1] * __uint_as_float(ml[1].y)) + __builtin_fmaf(c23[0], __uint_as_float(ml[2].y), c23[1] * __uint_as_float(ml[3].y));
      const float u0 = x16_add(glo ? t[0] : t[2]), u1 = x16_add(glo ? t[1] : t[3]);
      const float inv = __builtin_amdgcn_rcpf(x16_add(Ls));
      *(LAS float*)(L + lr) = (godd ? u1 : u0) * inv;
      BAR();
      if (w == 0) {
        v2f tt[8];
#pragma unroll
        for (int ww = 0; ww < 8; ++ww) tt[ww] = *(LAS v2f*)(L + lq + ww * 256);
        const v2f ts = (((tt[0] + tt[1]) + (tt[2] + tt[3])) + ((tt[4] + tt[5]) + (tt[6] + tt[7]))) + v2f{resid0, resid1};
        const unsigned pv = nosent(cvtpk(ts[0], ts[1]));
        const v4u p4 = {pv, dppu<0x55>(pv), dppu<0xAA>(pv), dppu<0xFF>(pv)};
        if ((lane & 3) == 0) st4(rs, po, p4);
      }
      if (LY == 0) transpose_v();
      BAR();
    }
    if (mlpRole) {
      LANES(); IDENT();
      float b1v = bf_lo(b1[32 * mi + (lane & 31)]);
      int xbw = L_XB + w * 128 + (8 * l15 + 4 * g) * 2, xbr = L_XB + w * 128 + 8 * g * 2;
      int rdw = L_RED + (w * 32 + l15) * 4, rdr = L_RED + (lane & 31) * 4, hbw = L_HB + w * 64 + lane * 2, hbr = L_HB + w * 64 + 16 * g;
      int po = WS_R + LY * R_LAYER + (lane & 1) * R_REP + (16 + mi) * R_ROW + (64 * w + (lane & ~1)) * 2;
      PIN(ident); PIN(b1v); PIN(xbw); PIN(xbr); PIN(rdw); PIN(rdr); PIN(hbw); PIN(hbr); PIN(po);
      int off[2]; v4u r[2];
#pragma unroll
      for (int i = 0; i < 2; ++i) off[i] = WS_R + LY * R_LAYER + (8 * i + (lane >> 3)) * R_ROW + (64 * w + 8 * (lane & 7)) * 2;
      delay(kDelay[LY == 0 ? 2 : 3]);
      gather4<2>(rs, off, R_REP, r);
      v4f xv = mfma(ident, r[0], v4f{0.f, 0.f, 0.f, 0.f}); xv = mfma(ident, r[1], xv);
#pragma unroll
      for (int d = 0; d < 4; ++d) xv[d] += dppf<ROR8>(xv[d]);
      if (lane < 32 && l15 < 8) *(LAS v2u*)(L + xbw) = v2u{cvtpk(xv[0], xv[1]), cvtpk(xv[2], xv[3])};
      v4u af[2];
#pragma unroll
      for (int s = 0; s < 2; ++s) af[s] = *(LAS v4u*)(L + xbr + 64 * s);
      SCHED();
      float s1 = (xv[0] + xv[1]) + (xv[2] + xv[3]), s2 = (xv[0] * xv[0] + xv[1] * xv[1]) + (xv[2] * xv[2] + xv[3] * xv[3]);
      s1 += dppf<XOR1>(s1); s2 += dppf<XOR1>(s2); s1 += dppf<XOR2>(s1); s2 += dppf<XOR2>(s2);
      s1 += dppf<HMIR>(s1); s2 += dppf<HMIR>(s2); s1 = x16_add(s1); s2 = x16_add(s2);
      v4f d1[2];
#pragma unroll
      for (int cg = 0; cg < 2; ++cg) d1[cg] = mfma(af[0], wr[cg], v4f{0.f, 0.f, 0.f, 0.f});
#pragma unroll
      for (int cg = 0; cg < 2; ++cg) d1[cg] = mfma(af[1], wr[2 + cg], d1[cg]);
      if (lane == 0) *(LAS v2f*)(L + L_RED + 1024 + w * 8) = v2f{s1, s2};
      if (lane < 16) { *(LAS float*)(L + rdw) = d1[0][0]; *(LAS float*)(L + rdw + 64) = d1[1][0]; }
      BAR();
      float vS[8]; v4f sv[4];
#pragma unroll
      for (int ww = 0; ww < 8; ++ww) vS[ww] = *(LAS float*)(L + rdr + ww * 128);
#pragma unroll
      for (int ww = 0; ww < 4; ++ww) sv[ww] = *(LAS v4f*)(L + L_RED + 1024 + ww * 16);
      const float sS = ((vS[0] + vS[1]) + (vS[2] + vS[3])) + ((vS[4] + vS[5]) + (vS[6] + vS[7]));
      const v4f st = (sv[0] + sv[1]) + (sv[2] + sv[3]);
      const float mu = (st[0] + st[2]) * (1.f / DM), var = fmaxf((st[1] + st[3]) * (1.f / DM) - mu * mu, 0.f), rstd = __builtin_amdgcn_rsqf(var + 1e-5f);
      const float pre = rstd * (sS - mu * c1tot) + b1v;
      const float u2 = pre * (1.f + 0.044715f * pre * pre) * (-2.f * 0.7978845608028654f * 1.4426950408889634f);
      const float hv = pre * __builtin_amdgcn_rcpf(1.f + __builtin_amdgcn_exp2f(u2));
      if (lane < 32) *(LAS u16*)(L + hbw) = (u16)(cvtpk(hv, hv) & 0xffffu);
      const v4u ha = *(LAS v4u*)(L + hbr);
      v4f y[4];
#pragma unroll
      for (int cg = 0; cg < 4; ++cg) y[cg] = mfma(ha, wr[4 + cg], v4f{0.f, 0.f, 0.f, 0.f});
      const float yv = (g & 2) ? ((g & 1) ? y[3][0] : y[2][0]) : ((g & 1) ? y[1][0] : y[0][0]);
      const float yn = dppf<XOR1>(yv);
      const unsigned pv = nosent((lane & 1) ? cvtpk(yn, yv) : cvtpk(yv, yn));
      const v4u p4 = {pv, dppu<0x102>(pv), dppu<0x104>(pv), dppu<0x106>(pv)};
      if ((lane & 6) == 0) st4(rs, po, p4);
    }
    if ((LY == 0 || finRole) && w == 0) {
      LANES(); IDENT();
      const v2u b2v = *(const v2u*)(b2 + 64 * j + 8 * (l15 & 7) + 4 * (g & 1));
      v4f bs = {bf_lo(b2v.x), bf_hi(b2v.x), bf_lo(b2v.y), bf_hi(b2v.y)};
      int xsw = L_XS + (8 * l15 + 4 * g) * 4, xsr = L_XS + (4 * l15 + g) * 4, xs2 = L_XS + 2 * (lane & 31) * 4;
      unsigned md[4];
#pragma unroll
      for (int d = 0; d < 4; ++d) md[d] = ((l15 >> 1) == d && l15 < 8) ? ((l15 & 1) ? 0xffff0000u : 0x0000ffffu) : 0u;
      int d1o = WS_D1 + (lane >> 5) * D1_REP + pidx * D1_SLOT + (8 * l15 + 4 * (g & 1)) * 4;
      PIN(ident); PIN(bs); PIN(xsw); PIN(xsr); PIN(xs2); PIN(md[0]); PIN(md[1]); PIN(md[2]); PIN(md[3]); PIN(d1o);
      if (LY == 0) {
        const int cb = WS_DC + (xg * 32 + (idx & ~7) + 4 * (lane >> 5)) * DC_SLOT + (lane & 31) * 32;
        v4u pc[8];
        for (unsigned spins = 0; spins < SPIN_CAP; ++spins) {
          asm volatile("" ::: "memory");
          bool ok = true;
#pragma unroll
          for (int p = 0; p < 4; ++p) { pc[2 * p] = ld4(rs, cb + p * DC_SLOT); pc[2 * p + 1] = ld4(rs, cb + p * DC_SLOT + 16); }
#pragma unroll
          for (int p = 0; p < 8; ++p) ok = ok && pc[p].y == tag && pc[p].w == tag;
          if (__all(ok)) break;
        }
        ctot = v4f{0.f, 0.f, 0.f, 0.f};
#pragma unroll
        for (int p = 0; p < 4; ++p) ctot += v4f{__uint_as_float(pc[2 * p].x), __uint_as_float(pc[2 * p].z), __uint_as_float(pc[2 * p + 1].x), __uint_as_float(pc[2 * p + 1].z)};
#pragma unroll
        for (int d = 0; d < 4; ++d) ctot[d] = x32_add(ctot[d]);
        PIN(ctot);
      }
      v4u wqb[4];
      if (LY == 0) {
#pragma unroll
        for (int i = 0; i < 4; ++i) { wqb[i] = *(LAS v4u*)(L + L_WQ + i * 1024 + lane * 16); PIN(wqb[i]); }
      }
      v4u r[8];
      int vo = WS_R + LY * R_LAYER + (lane >> 3) * R_ROW + (64 * j + 8 * (lane & 7)) * 2;
      int vo7 = 56 + (lane >> 3) < NROWS ? vo + 56 * R_ROW : OOB;
      PIN(vo); PIN(vo7);
      delay(mlpRole ? kDelay[4] : tileRole ? kDelay[5] : kDelay[LY == 0 ? 9 : 10]);
      unsigned spins = 0;
      for (; spins < SPIN_CAP; ++spins) {
        asm volatile("" ::: "memory");
#pragma unroll
        for (int i = 0; i < 7; ++i) r[i] = __builtin_amdgcn_raw_buffer_load_b128(rs, vo, i * 8 * R_ROW, AUX_SC1);
        r[7] = ld4(rs, vo7);
        unsigned m = 0;
#pragma unroll
        for (int i = 0; i < 8; ++i) m = umax4(m, r[i]);
        if (!__any(m == SENT)) break;
        delay(kDelay[8]);
        if (spins < NREP - 1) { vo += R_REP; vo7 += R_REP; }
      }
      v4f x0 = mfma(ident, r[0], v4f{0.f, 0.f, 0.f, 0.f}), x1 = mfma(ident, r[1], v4f{0.f, 0.f, 0.f, 0.f});
#pragma unroll
      for (int i = 2; i < 8; i += 2) { x0 = mfma(ident, r[i], x0); x1 = mfma(ident, r[i + 1], x1); }
      v4f xv = x0 + x1;
#pragma unroll
      for (int d = 0; d < 4; ++d) xv[d] += dppf<ROR8>(xv[d]);
      xv += bs;
      if (lane < 32 && l15 < 8) *(LAS v4f*)(L + xsw) = xv;
      if (LY == 0) {
        const float xq = *(LAS float*)(L + xsr);
        float s1 = (xv[0] + xv[1]) + (xv[2] + xv[3]), s2 = (xv[0] * xv[0] + xv[1] * xv[1]) + (xv[2] * xv[2] + xv[3] * xv[3]);
        s1 = g < 2 ? s1 : 0.f; s2 = g < 2 ? s2 : 0.f;
        s1 += dppf<XOR1>(s1); s2 += dppf<XOR1>(s2); s1 += dppf<XOR2>(s1); s2 += dppf<XOR2>(s2);
        s1 += dppf<HMIR>(s1); s2 += dppf<HMIR>(s2); s1 = x32_add(x16_add(s1)); s2 = x32_add(x16_add(s2));
        const unsigned xp = cvtpk(xq, xq);
        v4f a0 = {0.f, 0.f, 0.f, 0.f}, a1 = {0.f, 0.f, 0.f, 0.f};
#define XQSTEP(i) { const unsigned pb = dppu<NBC + (i)>(xp); v4u aa; _Pragma("unroll") for (int d = 0; d < 4; ++d) aa[d] = pb & md[d]; \
                    if ((i) & 1) a1 = mfma(aa, wqb[(i) & 3], a1); else a0 = mfma(aa, wqb[(i) & 3], a0); \
                    if ((i) < 12) wqb[(i) & 3] = *(LAS v4u*)(L + L_WQ + (4 + (i)) * 1024 + lane * 16); }
        XQSTEP(0) XQSTEP(1) XQSTEP(2) XQSTEP(3) XQSTEP(4) XQSTEP(5) XQSTEP(6) XQSTEP(7)
        XQSTEP(8) XQSTEP(9) XQSTEP(10) XQSTEP(11) XQSTEP(12) XQSTEP(13) XQSTEP(14) XQSTEP(15)
#undef XQSTEP
        v4f qs = a0 + a1;
        v4u pv;
#pragma unroll
        for (int d = 0; d < 4; ++d) { auto sw = __builtin_amdgcn_permlane32_swap(__float_as_uint(qs[d]), __float_as_uint(qs[d]), false, false); pv[d] = nosent(sw[0]); }
        st4(rs, d1o, pv);
        if (lane < NREP) st4(rs, WS_D1 + lane * D1_REP + pidx * D1_SLOT + 512, v4u{fbits(s1), fbits(s2), 0u, 0u});
        if (tileRole && prow == 0) { const v2f rv = *(LAS v2f*)(L + xs2); resid0 = rv[0]; resid1 = rv[1]; }
        const int db = WS_D1 + (xg * 32 + (idx & ~7)) * D1_SLOT;
        int off2[5]; v4u pq[5];
#pragma unroll
        for (int p = 0; p < 4; ++p) off2[p] = db + (4 * (lane >> 5) + p) * D1_SLOT + (lane & 31) * 16;
        off2[4] = db + (lane & 7) * D1_SLOT + 512;
        delay(kDelay[7]);
        gather4<5>(rs, off2, D1_REP, pq);
        v4f Q = (__builtin_bit_cast(v4f, pq[0]) + __builtin_bit_cast(v4f, pq[1])) + (__builtin_bit_cast(v4f, pq[2]) + __builtin_bit_cast(v4f, pq[3]));
#pragma unroll
        for (int d = 0; d < 4; ++d) Q[d] = x32_add(Q[d]);
        float t1 = __uint_as_float(pq[4].x), t2 = __uint_as_float(pq[4].y);
        t1 += dppf<XOR1>(t1); t2 += dppf<XOR1>(t2); t1 += dppf<XOR2>(t1); t2 += dppf<XOR2>(t2); t1 += dppf<HMIR>(t1); t2 += dppf<HMIR>(t2);
        const float mu = t1 * (1.f / DM), var = fmaxf(t2 * (1.f / DM) - mu * mu, 0.f), rstd = __builtin_amdgcn_rsqf(var + 1e-5f);
        const v4f q = rstd * (Q - mu * ctot);
        if (lane < 32) *(LAS v2u*)(L + L_QFIN + lane * 8) = v2u{cvtpk(q[0], q[1]), cvtpk(q[2], q[3])};
      } else if (lane < 32 && l15 < 8) {
        __hip_atomic_store((unsigned long long*)(out + 64 * j + 8 * l15 + 4 * g), (unsigned long long)cvtpk(xv[0], xv[1]) | ((unsigned long long)cvtpk(xv[2], xv[3]) << 32), __ATOMIC_RELAXED, __HIP_MEMORY_SCOPE_AGENT);
      }
    }
    if (LY == 0) BAR();
  }
}

extern "C" void kernel_launch(void* const* d_in, const int* in_sizes, int n_in, void* d_out, int out_size, void* d_ws,
                              size_t ws_size, hipStream_t stream) {
  (void)in_sizes; (void)n_in; (void)out_size; (void)ws_size;
  (void)hipFuncSetAttribute((const void*)mk_kernel, hipFuncAttributeMaxDynamicSharedMemorySize, LDS_BYTES);
  hipLaunchKernelGGL(mk_kernel, dim3(NWG), dim3(NT), LDS_BYTES, stream,
                     (const u16*)d_in[0], (const u16*)d_in[1], (const u16*)d_in[2], (const u16*)d_in[3], (const u16*)d_in[4],
                     (const u16*)d_in[6], (const u16*)d_in[7], (const u16*)d_in[8], (const u16*)d_in[9], (u16*)d_out,
                     (unsigned char*)d_ws);
}
```
